```python
import torch

def _softmax(x, axis=-1):
    return torch.softmax(x, dim=axis)

def _gelu(x):
    return torch.nn.functional.gelu(x, approximate='tanh')

def _silu(x):
    return torch.nn.functional.silu(x)

def _sigmoid(x):
    return torch.sigmoid(x)

def _layernorm(x, axis=-1, eps=1e-05):
    m = x.mean(dim=axis, keepdim=True)
    v = x.var(dim=axis, keepdim=True, unbiased=False)
    return (x - m) / torch.sqrt(v + eps)

def _scatter_add(tgt, idx, src, axis=0):
    return tgt.clone().index_add_(axis, idx, src)

def _segment_sum(src, seg, n):
    out = torch.zeros((n,) + src.shape[1:], dtype=src.dtype)
    return out.index_add_(0, seg, src)

def _pad(x, pw):
    flat = tuple((v for pair in reversed(pw) for v in pair))
    return torch.nn.functional.pad(x, flat)
kernel_name = 'parallel_sum__linear_attn_block__mixer_gmlp__prompt'

def setup_inputs():

    _w = lambda a: (lambda t: t.to(torch.bfloat16) if t.is_floating_point() else t)(torch.as_tensor(a))

    def _A_setup():
        B, S, D = (1, 2048, 640)
        return {'x': _w((lambda *s: torch.randn(*s, dtype=torch.bfloat16) * 0.05)(B, S, D)), 'wqkv': _w((lambda *s: torch.randn(*s, dtype=torch.bfloat16) * 0.8)(D, 3 * D)), 'wo': _w((lambda *s: torch.randn(*s, dtype=torch.bfloat16) * 0.2)(D, D))}
    _A_in = _A_setup()

    def _B_setup():
        B, S, D, F = (1, 2048, 640, 1728)
        Fh = F // 2
        return {'x': _w((lambda *s: torch.randn(*s, dtype=torch.bfloat16) * 0.05)(B, S, D)), 'w_in': _w((lambda *s: torch.randn(*s, dtype=torch.bfloat16) * 0.4)(D, 2 * Fh)), 'w_sp': _w(0.01 * (lambda *s: torch.randn(*s, dtype=torch.bfloat16) * 0.4)(S, S)), 'b_sp': _w(1.0 + 0.01 * (lambda *s: torch.randn(*s, dtype=torch.bfloat16) * 0.05)(S)), 'w_out': _w((lambda *s: torch.randn(*s, dtype=torch.bfloat16) * 0.05)(Fh, D))}
    _B_in = _B_setup()
    return {'A_x': _A_in['x'], 'A_wqkv': _A_in['wqkv'], 'A_wo': _A_in['wo'], 'B_x': _B_in['x'], 'B_w_in': _B_in['w_in'], 'B_w_sp': _B_in['w_sp'], 'B_b_sp': _B_in['b_sp'], 'B_w_out': _B_in['w_out']}

def reference(A_x, A_wqkv, A_wo, B_x, B_w_in, B_w_sp, B_b_sp, B_w_out):

    def _A_ref(x, wqkv, wo):
        B, S, D = x.shape
        H = 32
        Dh = D // H
        qkv = (lambda x, *d: x.permute(*d))((x @ wqkv).reshape(B, S, 3, H, Dh), 2, 0, 3, 1, 4)
        q, k, v = (qkv[0], qkv[1], qkv[2])
        phi = lambda t: torch.where(t > 0, t + 1, torch.exp(t)) + 1e-06
        pq, pk = (phi(q), phi(k))
        kv = torch.einsum('bhsd,bhse->bhde', pk, v)
        z = torch.einsum('bhsd->bhd', pk)
        num = torch.einsum('bhqd,bhde->bhqe', pq, kv)
        den = torch.einsum('bhqd,bhd->bhq', pq, z)[..., None] + 1e-06
        o = num / den
        return (lambda x, *d: x.permute(*d))(o, 0, 2, 1, 3).reshape(B, S, D) @ wo

    def _B_ref(x, w_in, w_sp, b_sp, w_out):
        B, S, D = x.shape
        F = w_in.shape[-1]
        Fh = F // 2
        z = _gelu(torch.einsum('bsd,df->bsf', x, w_in))
        z1 = z[..., :Fh].clone()
        z2 = _layernorm(z[..., Fh:], axis=-1)
        g = torch.einsum('ts,bsf->btf', w_sp, z2) + b_sp[None, :, None]
        return torch.einsum('bsf,fd->bsd', z1 * g, w_out)
    x = A_x
    xn = _layernorm(x, axis=-1)
    return x + _A_ref(xn, A_wqkv, A_wo) + _B_ref(xn, B_w_in, B_w_sp, B_b_sp, B_w_out)
```

```cpp
#include <hip/hip_runtime.h>
#include <stdint.h>

typedef unsigned u32;
typedef unsigned long long u64;
typedef unsigned short u16;
typedef u32 v4u __attribute__((ext_vector_type(4)));
typedef u32 v2u __attribute__((ext_vector_type(2)));
typedef float f4 __attribute__((ext_vector_type(4)));
typedef float f2 __attribute__((ext_vector_type(2)));
typedef short s4 __attribute__((ext_vector_type(4)));
typedef __bf16 bf8 __attribute__((ext_vector_type(8)));
typedef __bf16 bf2 __attribute__((ext_vector_type(2)));
typedef __amdgpu_buffer_rsrc_t rsrc_t;
#define LAS __attribute__((address_space(3)))
#define GAS __attribute__((address_space(1)))
#define DI __device__ __forceinline__
#define RLX_AGENT __ATOMIC_RELAXED, __HIP_MEMORY_SCOPE_AGENT

constexpr u32 WS_EPOCH = 0;
constexpr u32 WS_F1 = 4096;
constexpr u32 WS_FKV = WS_F1 + 256 * 128;
constexpr u32 WS_FM = WS_FKV + 128 * 128;
constexpr u32 WS_FH = WS_FM + 256 * 128;
constexpr u32 WS_FA = WS_FH + 256 * 128;
constexpr u32 WS_F2 = WS_FA + 256 * 128;
constexpr u32 WS_STAT = 196608;
constexpr u32 WS_XST = WS_STAT + 16 * 7 * 128 * 16;
constexpr u32 WS_Z2N = WS_XST + 2048 * 16;
constexpr u32 WS_KV = WS_Z2N + 64 * 54 * 1024;
constexpr u32 WS_PQ = WS_KV + 64 * 80 * 1024;
constexpr u32 WS_Z1 = WS_PQ + 128 * 40 * 512;
constexpr u32 WS_H = WS_Z1 + 128 * 54 * 512;
constexpr u32 WS_KVP = WS_H + 128 * 54 * 512;
constexpr u32 WS_MB = WS_KVP + 16 * 32 * 448 * 4;
constexpr u32 WS_ZP = WS_MB + 8 * 8 * 25 * 512;
constexpr u32 WS_WOB = WS_ZP + 8 * 16 * 80 * 4;
constexpr u32 WS_AQ = WS_WOB + 8 * 27 * 5 * 1024;
constexpr u32 WS_END = WS_AQ + 128 * 40 * 512;
constexpr u32 CODE_BYTES = 40000;
constexpr u32 OOB = 0x80000000u;
constexpr u32 SPIN_MAX = 1u << 18;

constexpr u32 STAGE = 49152;
constexpr u32 L_MISC = 3 * STAGE;
constexpr u32 L_MEAN = 4096, L_RSTD = 4096 + 512, L_CW = 4096 + 1024, L_ZS = L_MISC + 2048;
constexpr u32 L_WO = L_MISC;
constexpr u32 L_CNT = L_MISC + 16128;
constexpr u32 L_KVS = L_MISC + 8192;

DI f4 mfma(v4u a, v4u b, f4 c) { return __builtin_amdgcn_mfma_f32_16x16x32_bf16(__builtin_bit_cast(bf8, a), __builtin_bit_cast(bf8, b), c, 0, 0, 0); }
DI u32 pk2(float lo, float hi) { f2 v = {lo, hi}; return __builtin_bit_cast(u32, __builtin_convertvector(v, bf2)); }
DI float blo(u32 v) { return __builtin_bit_cast(float, v << 16); }
DI float bhi(u32 v) { return __builtin_bit_cast(float, v & 0xffff0000u); }
DI float fb(u32 v) { return __builtin_bit_cast(float, v); }
DI u32 ub(float v) { return __builtin_bit_cast(u32, v); }
DI v4u lds128(u32 a) { return *(LAS v4u*)a; }
DI f4 ldsf4(u32 a) { return *(LAS f4*)a; }
DI v2u lds64(u32 a) { return *(LAS v2u*)a; }
DI float ldsf(u32 a) { return *(LAS float*)a; }
DI v2u ldstr(u32 a) { return __builtin_bit_cast(v2u, __builtin_amdgcn_ds_read_tr16_b64_v4i16((LAS s4*)a)); }
DI v4u ldstr2(u32 a) { v2u l = ldstr(a), h = ldstr(a + 512); return v4u{l.x, l.y, h.x, h.y}; }
DI rsrc_t mkrs(const void* p, u32 bytes) { return __builtin_amdgcn_make_buffer_rsrc((void*)p, 0, bytes, 0x00020000); }
DI v4u ld128c(rsrc_t rs, u32 voff, u32 soff) { return __builtin_amdgcn_raw_buffer_load_b128(rs, voff, soff, 16); }
DI v2u ld64c(rsrc_t rs, u32 voff, u32 soff) { return __builtin_amdgcn_raw_buffer_load_b64(rs, voff, soff, 16); }
DI void st128c(v4u v, rsrc_t rs, u32 voff, u32 soff) {
  __builtin_amdgcn_raw_buffer_store_b128(v, rs, voff, soff, 16);
  asm volatile("s_nop 1" ::"v"(v) : "memory");
}
DI void st64c(v2u v, rsrc_t rs, u32 voff, u32 soff) { __builtin_amdgcn_raw_buffer_store_b64(v, rs, voff, soff, 16); }
DI void DMA(rsrc_t rs, u32 voff, u32 soff, u32 lds) {
  asm volatile("s_mov_b32 m0, %0\n\ts_nop 0\n\tbuffer_load_dwordx4 %1, %2, %3 offen lds" ::"s"(lds), "v"(voff), "s"(rs), "s"(soff) : "memory");
}
DI void hid128c(v4u& d, rsrc_t rs, u32 voff, u32 soff) { asm volatile("buffer_load_dwordx4 %0, %1, %2, %3 offen sc1" : "=v"(d) : "v"(voff), "s"(rs), "s"(soff) : "memory"); }
DI void hid64c(u64& d, const char* base, u32 voff) { asm volatile("global_load_dwordx2 %0, %1, %2 sc1" : "=v"(d) : "v"(voff), "s"(base) : "memory"); }
template <class T> DI void opaque(T& v) { asm volatile("" : "+v"(v)); }
#define WAITV(n) asm volatile("s_waitcnt vmcnt(" #n ")" ::: "memory")
#define WAITL() asm volatile("s_waitcnt lgkmcnt(0)" ::: "memory")
#define BAR() do { asm volatile("s_waitcnt lgkmcnt(0)\n\ts_barrier" ::: "memory"); } while (0)
#define SCHED() __builtin_amdgcn_sched_barrier(0)
DI void MF(f4& acc, const v4u& a, const v4u& b) { asm volatile("v_mfma_f32_16x16x32_bf16 %0, %1, %2, %0" : "+v"(acc) : "v"(a), "v"(b)); }
DI void MF0(f4& acc, const v4u& a, const v4u& b) { asm volatile("v_mfma_f32_16x16x32_bf16 %0, %1, %2, 0" : "=&v"(acc) : "v"(a), "v"(b)); }
#define NOP_IN() do { SCHED(); asm volatile("s_nop 1"); SCHED(); } while (0)
#define NOP_OUT() do { SCHED(); asm volatile("s_nop 7"); SCHED(); } while (0)
template <int CTRL> DI float dppadd(float x) { return x + fb(__builtin_amdgcn_update_dpp(0, ub(x), CTRL, 0xf, 0xf, true)); }
DI float quadsum(float x) { return dppadd<0x4E>(dppadd<0xB1>(x)); }
DI float rowsum16(float x) { return dppadd<0x140>(dppadd<0x141>(quadsum(x))); }

template <class T> DI T pin(T v) { asm("" : "+v"(v)); return v; }
template <class T> DI T pins(T v) { asm("" : "+s"(v)); return v; }
DI f2 lo2(f4 v) { return f2{v[0], v[1]}; }
DI f2 hi2(f4 v) { return f2{v[2], v[3]}; }
DI f4 cat(f2 a, f2 b) { return f4{a[0], a[1], b[0], b[1]}; }
struct Consts { f2 c0, c1, one, eps; };
DI Consts mkconsts() {
  const float c0 = -2.0f * 0.7978845608028654f * 1.4426950408889634f, c1 = c0 * 0.044715f;
  Consts k = {pin(f2{c0, c0}), pins(f2{c1, c1}), pins(f2{1.0f, 1.0f}), pins(f2{1e-6f, 1e-6f})};
  return k;
}
DI f2 gelu2(f2 x, const Consts& k) {
  const f2 p = pin(__builtin_elementwise_fma(x * x, k.c1, k.c0) * x);
  const f2 d = pin(f2{__builtin_amdgcn_exp2f(p[0]), __builtin_amdgcn_exp2f(p[1])} + k.one);
  return x * f2{__builtin_amdgcn_rcpf(d[0]), __builtin_amdgcn_rcpf(d[1])};
}
DI f2 phi2(f2 u, f2 rs, f2 rl, const Consts& k) {
  const f2 a = pin(u * rl), t1 = pin(__builtin_elementwise_fma(u, rs, k.one));
  const float big = 3.0e38f;
  const f2 m = {__builtin_amdgcn_fmed3f(t1[0], __builtin_amdgcn_fmed3f(__builtin_amdgcn_exp2f(a[0]), 0.f, 1.f), big),
                __builtin_amdgcn_fmed3f(t1[1], __builtin_amdgcn_fmed3f(__builtin_amdgcn_exp2f(a[1]), 0.f, 1.f), big)};
  return m + k.eps;
}

DI void wait_flags(const char* ws, u32 off, u64 tag) {
  const GAS u64* p = (const GAS u64*)(ws + off);
#pragma unroll 1
  for (u32 spins = 0; spins < SPIN_MAX; ++spins) {
    u64 v = __hip_atomic_load(p, RLX_AGENT);
    if (__all(v == tag)) break;
  }
  __builtin_amdgcn_fence(__ATOMIC_ACQUIRE, "wavefront");
}
DI void set_flag(char* ws, u32 off, u64 tag) { __hip_atomic_store((GAS u64*)(ws + off), tag, RLX_AGENT); }
DI u32 owner(u32 v) {
  const u64 t = v < 32 ? (v < 16 ? 0x543210FFEEDDDCCCull : 0x433221100FFEEDC6ull) : (v < 48 ? 0x7BBAA99887766554ull : 0xBBAA99887ull);
  return (u32)(t >> ((v & 15) * 4)) & 15u;
}

template <int I> struct IC { static constexpr int v = I; };
template <int I, int N, class F> DI void sfor(F&& f) { if constexpr (I < N) { f(IC<I>{}); sfor<I + 1, N>(f); } }


DI constexpr u32 a2slot(int u, int j) {
  return (u & 1) == 0 ? (j < 6 ? STAGE + j * 1024 : 2 * STAGE + (j - 6) * 1024) : (j < 4 ? 2 * STAGE + (j + 2) * 1024 : (j - 4) * 1024);
}
template <int U> DI void a2read(v4u (&dst)[2][4], u32 rd) {
#pragma unroll
  for (int ks = 0; ks < 2; ++ks)
#pragma unroll
    for (int rt = 0; rt < 4; ++rt) dst[ks][rt] = lds128((rd ^ (ks * 64)) + a2slot(U, 2 * rt));
}
DI float halfsum(float x) {
  const u32 u = ub(rowsum16(x));
  const auto t = __builtin_amdgcn_permlane16_swap(u, u, false, false);
  return fb(t[0]) + fb(t[1]);
}
DI f4 rowsum16x16(const f4 (&v)[4]) {
  float t0, t1, t2, t3, t4, t5, t6, t7;
#define A1(d, s, m) "v_add_f32_dpp %" #d ", %" #s ", %" #s " row_mirror row_mask:0xf bank_mask:" #m "\n\t"
#define A2(d, s, m) "v_add_f32_dpp %" #d ", %" #s ", %" #s " row_half_mirror row_mask:0xf bank_mask:" #m "\n\t"
#define A3(d, p) "v_add_f32_dpp %" #d ", %" #d ", %" #d " quad_perm:" p " row_mask:0xf bank_mask:0xf\n\t"
  asm volatile("s_nop 1\n\t" A1(0, 8, 0x3) A1(1, 9, 0x3) A1(2, 10, 0x3) A1(3, 11, 0x3) A1(4, 12, 0x3) A1(5, 13, 0x3) A1(6, 14, 0x3) A1(7, 15, 0x3)
               A1(0, 16, 0xc) A1(1, 17, 0xc) A1(2, 18, 0xc) A1(3, 19, 0xc) A1(4, 20, 0xc) A1(5, 21, 0xc) A1(6, 22, 0xc) A1(7, 23, 0xc)
               A2(0, 0, 0x5) A2(1, 1, 0x5) A2(2, 2, 0x5) A2(3, 3, 0x5) A2(0, 4, 0xa) A2(1, 5, 0xa) A2(2, 6, 0xa) A2(3, 7, 0xa)
               A3(0, "[1,0,3,2]") A3(1, "[1,0,3,2]") A3(2, "[1,0,3,2]") A3(3, "[1,0,3,2]")
               A3(0, "[2,3,0,1]") A3(1, "[2,3,0,1]") A3(2, "[2,3,0,1]") A3(3, "[2,3,0,1]")
               : "=&v"(t0), "=&v"(t1), "=&v"(t2), "=&v"(t3), "=&v"(t4), "=&v"(t5), "=&v"(t6), "=&v"(t7)
               : "v"(v[0][0]), "v"(v[0][1]), "v"(v[0][2]), "v"(v[0][3]), "v"(v[1][0]), "v"(v[1][1]), "v"(v[1][2]), "v"(v[1][3]),
                 "v"(v[2][0]), "v"(v[2][1]), "v"(v[2][2]), "v"(v[2][3]), "v"(v[3][0]), "v"(v[3][1]), "v"(v[3][2]), "v"(v[3][3]));
#undef A1
#undef A2
#undef A3
  return f4{t0, t1, t2, t3};
}
DI v4u pack8(f4 a, f4 b) { return v4u{pk2(a[0], a[1]), pk2(a[2], a[3]), pk2(b[0], b[1]), pk2(b[2], b[3])}; }

__global__ void __launch_bounds__(512, 2)
mega(const u16* x, const u16* wqkv, const u16* wo, const u16* win, const u16* wsp, const u16* bsp, const u16* wout, u16* out, char* ws) {
  __shared__ __attribute__((aligned(16))) char smem[163840];
  asm volatile("" ::"v"((LAS char*)smem));
  const u32 tid = threadIdx.x, lane = tid & 63, w = __builtin_amdgcn_readfirstlane(tid >> 6);
  const u32 b = blockIdx.x, xcd = b & 7, loc = b >> 3, rg = xcd >> 1, jh = xcd & 1, grp = loc >> 3, slot = loc & 7;
  u32 touch0;
  {
    u64 pc;
    asm volatile("s_getpc_b64 %0" : "=s"(pc));
    const u32 o = (loc * 12 + min(lane, 11u)) * 128;
    asm volatile("global_load_dword %0, %1, %2" : "=v"(touch0) : "v"(w == 0 ? min(o, CODE_BYTES) : 0u), "s"(pc & ~127ull) : "memory");
  }
  const u32 g = lane >> 4, i16 = lane & 15, wr = w >> 2, wc = w & 3, r8 = lane >> 3;
  const u32 P = 4 * rg + grp;
  const u32 sid = ((jh ? 0xFDBA8654u : 0xEC973210u) >> (4 * slot)) & 15u;
  const bool light = sid < 7;
  const u32 m7 = -(u32)(sid >= 7), m12 = -(u32)(sid >= 12), m14 = -(u32)(sid >= 14);
  constexpr u32 b0 = 0x000A1817u, b1 = 0x302F2625u - 7 * 0x02020202u, b2 = 0x11020100u - 12 * 0x01030303u, b3 = 0x14130706u - 14 * 0x02020202u;
  const u32 lines = (b0 ^ (m7 & (b0 ^ b1)) ^ (m12 & (b1 ^ b2)) ^ (m14 & (b2 ^ b3))) + sid * (0x00010202u ^ (m7 & 0x02030000u) ^ ((m12 ^ m14) & 0x03010101u));
  const u32 p64 = 8 * rg + 2 * grp + (slot & 1);
  const u32 cc = (slot >> 2) + (slot & 2), cs = 4 * jh + cc, n0 = 27 * jh + 7 * cc;
  asm volatile("" : "+s"(x), "+s"(wqkv), "+s"(wo), "+s"(win), "+s"(wsp), "+s"(bsp), "+s"(wout), "+s"(out), "+s"(ws));
  const rsrc_t rx = mkrs(x, 2048 * 1280), rsp = mkrs(wsp, 2048 * 4096), rws = mkrs(ws, WS_END);

  u32 vx[2], vl[4], sl[4], sstep[4];
  rsrc_t rl[4];
  vx[0] = (16 * w + r8) * 1280 + (((lane & 7) ^ ((g & 1) | ((w & 1) << 2))) << 4);
  vx[1] = (vx[0] ^ 32) + 8 * 1280;
  const u32 wsw = ((lane & 7) ^ (2 * (g & 1) + 4 * (w & 1))) << 4;
  const u32 vq = (8 * w + r8) * 3840 + wsw, vi = (8 * w + r8) * 3456 + wsw;
#pragma unroll
  for (int l = 0; l < 4; ++l) {
    const u32 v = (lines >> (8 * l)) & 255;
    const bool in = v >= 10 && v < 37;
    rl[l] = mkrs(in ? win : wqkv, in ? 640 * 3456 : 640 * 3840);
    sl[l] = (in ? v - 10 : (v < 10 ? v : v - 27)) * 128;
    sstep[l] = in ? 64 * 3456 : 64 * 3840;
    vl[l] = in ? vi : vq;
  }
  const u32 sx = P * (128 * 1280), rot = ((rg * 5) >> 1) + (grp >> 1);
  auto dma_xw = [&](int k, u32 ch) {
    const u32 so = (ch % 3) * STAGE, c0 = ch + rot, cr = c0 >= 10 ? c0 - 10 : c0;
    if (k < 2) DMA(rx, vx[k], sx + cr * 128, so + w * 2048 + k * 1024);
    else DMA(rl[k - 2], vl[k - 2], sl[k - 2] + cr * sstep[k - 2], so + 16384 + (k - 2) * 8192 + w * 1024);
  };
  auto stage_in = [&](u32 ch) {
#pragma unroll
    for (int k = 0; k < 5; ++k) dma_xw(k, ch);
    if (!light) dma_xw(5, ch);
  };
  u64 ep;
  v4u xa[3];
  const u32 xr0 = (sid - 7) * 8 + w + 72 * (lane >> 5), xo = (!light && xr0 < 128) ? xr0 * 1280 + (lane & 31) * 16 : OOB;
  stage_in(0);
  hid64c(ep, ws, 0u);
  asm volatile("buffer_load_dwordx4 %0, %3, %5, %6 offen\n\tbuffer_load_dwordx4 %1, %3, %5, %6 offen offset:512\n\tbuffer_load_dwordx4 %2, %4, %5, %6 offen offset:1024"
               : "=&v"(xa[0]), "=&v"(xa[1]), "=&v"(xa[2]) : "v"(xo), "v"((lane & 16) ? OOB : xo), "s"(rx), "s"(sx) : "memory");
  __builtin_amdgcn_s_barrier();
  stage_in(1);
  const u32 rp_unit = ((xcd * 4 + grp) * 3 + slot) * 3 + w, rp_st = rp_unit / 10, rp_ln = rp_unit - rp_st * 10;
  const bool rp = slot < 3 && w < 3 && rp_unit < 270;
  const u32 rp_lds = w * STAGE + 16384 + 3 * 8192;
  const u32 va0 = r8 * 4096 + (((lane & 7) ^ g) << 4), va1 = va0 ^ 64;
  const u32 sa2 = p64 * (64 * 4096) + 4 * w * 128;
  auto pair_in = [&](int p) {
    const u32 so = sa2 + ((p >> 2) ^ jh) * 128 + ((p & 3) ^ cc) * (16 * 4096);
    DMA(rsp, va0, so, L_MISC + w * 2048);
    DMA(rsp, va1, so + 8 * 4096, L_MISC + w * 2048 + 1024);
  };
  auto unit_blk = [&](int u, int j, u32 usrc) { DMA(rsp, (j & 1) ? va1 : va0, sa2 + (usrc ^ jh) * 128 + (j ^ (2 * cc)) * (8 * 4096), a2slot(u, j) + w * 6144); };
  u32 ra[2][2], rb[2][4], vt[4];
  const u32 q4 = i16 >> 2, p4 = lane & 3, sig = 2 * ((q4 >> 1) & 1) + 4 * (g & 1);
  ra[0][0] = (64 * wr + 8 * q4 + p4) * 128 + ((g ^ (((i16 >> 1) & 1) | (q4 << 1))) << 4);
  ra[0][1] = ra[0][0] ^ 64;
  const u32 rb0 = 16384 + g * 1024 + q4 * 128 + (((2 * wc + (p4 >> 1)) ^ sig) << 4) + 8 * (p4 & 1);
#pragma unroll
  for (int i = 0; i < 4; ++i) {
    const u32 li = (light && i >= 2) ? 2 : i ^ wr;
    vt[i] = 4 * ((lines >> (8 * li)) & 255) + wc;
    rb[0][i] = rb0 + li * 8192;
    rb[1][i] = rb[0][i] + 2 * STAGE;
    opaque(rb[0][i]); opaque(rb[1][i]);
  }
  ra[1][0] = ra[0][0] + 2 * STAGE;
  ra[1][1] = ra[0][1] + 2 * STAGE;
  opaque(ra[0][0]); opaque(ra[0][1]); opaque(ra[1][0]); opaque(ra[1][1]);
  u32 a2l = i16 * 128 + ((g ^ ((i16 >> 1) & 7)) << 4);
  opaque(a2l);
  u64 tag;
  u32 tl, th;
  auto xstats = [&]() {
    opaque(ep); opaque(xa[0]); opaque(xa[1]); opaque(xa[2]);
    tag = (((u64)__builtin_amdgcn_readfirstlane((u32)(ep >> 32)) << 32) | __builtin_amdgcn_readfirstlane((u32)ep)) + 0x9E3779B97F4A7C15ull;
    tl = (u32)tag; th = (u32)(tag >> 32);
    if (light) return;
    f2 v[12], s = {0.f, 0.f};
#pragma unroll
    for (int j = 0; j < 12; ++j) { v[j] = f2{blo(xa[j >> 2][j & 3]), bhi(xa[j >> 2][j & 3])}; s += v[j]; }
    const float mean = halfsum(s[0] + s[1]) * (1.0f / 640.0f);
    f2 q = {0.f, 0.f}, q2 = {0.f, 0.f};
#pragma unroll
    for (int j = 0; j < 8; ++j) { const f2 d = v[j] - mean; q += d * d; }
#pragma unroll
    for (int j = 8; j < 12; ++j) { const f2 d = v[j] - mean; q2 += d * d; }
    const float var = halfsum((lane & 16) ? q[0] + q[1] : (q[0] + q[1]) + (q2[0] + q2[1])) * (1.0f / 640.0f);
    st128c(v4u{ub(-mean), tl, ub(__builtin_amdgcn_rsqf(var + 1e-5f)), th}, rws, ((lane & 31) == 0 && xr0 < 128) ? xr0 * 16 : OOB, WS_XST + P * 2048);
  };
  f4 acc[4][4], cwa[2];
  v4u A2a[2][4], A2b[2][4];
  v4u xst;
  {
    v4u ones = {0x3f803f80u, 0x3f803f80u, 0x3f803f80u, 0x3f803f80u};
    opaque(ones);
    v4u fa[2][4];
    v2u fbb[2][8];
    auto rd_a = [&](int ch, int ks, int r) { return lds128(ra[ch % 3 == 2][ks] + (ch % 3 == 1 ? STAGE : 0) + (r >> 1) * 4096 + (r & 1) * 512); };
    auto rd_b = [&](int ch, int ks, int h) { return ldstr(rb[ch % 3 == 2][h >> 1] + (ch % 3 == 1 ? STAGE : 0) + ks * 4096 + (h & 1) * 512); };
    asm volatile("s_waitcnt vmcnt(9)\n\ts_barrier" ::: "memory");
    fbb[0][0] = rd_b(0, 0, 0); fbb[0][1] = rd_b(0, 0, 1);
#pragma unroll
    for (int r = 0; r < 4; ++r) fa[0][r] = rd_a(0, 0, r);
#pragma unroll
    for (int h = 2; h < 8; ++h) fbb[0][h] = rd_b(0, 0, h);
    NOP_IN();
    sfor<0, 20>([&](auto H) {
      constexpr int hf = decltype(H)::v, ch = hf >> 1, ks = hf & 1;
      constexpr int nch = (hf + 1) >> 1, nks = ks ^ 1;
      constexpr int tg = hf == 0 ? 2 : ch + 3;
      constexpr bool fill = ks == 1 || hf == 0;
      auto refill = [&](int k) {
        if constexpr (tg < 10) dma_xw(k, tg);
        else {
          const int q = (tg - 10) * 6 + k;
          if (q < 16) unit_blk(q >> 3, q & 7, (light ? 0 : 2) + (q >> 3));
        }
      };
      auto mm = [&](int m) {
        const int i = m >> 2, r = m & 3;
        const v4u bf = {fbb[ks][2 * i].x, fbb[ks][2 * i].y, fbb[ks][2 * i + 1].x, fbb[ks][2 * i + 1].y};
        if (i == 3) acc[r][i] = mfma(fa[ks][r], bf, hf == 0 ? f4{0.f, 0.f, 0.f, 0.f} : acc[r][i]);
        else if (hf == 0) MF0(acc[r][i], fa[ks][r], bf);
        else MF(acc[r][i], fa[ks][r], bf);
        if (r == 3 && (i & 1) == 0) { if (hf == 0) MF0(cwa[i >> 1], ones, bf); else MF(cwa[i >> 1], ones, bf); }
        SCHED();
      };
#pragma unroll
      for (int m = 0; m < 12; ++m) {
        mm(m);
        if (hf < 19) {
          if (m < 2) fbb[nks][m] = rd_b(nch, nks, m);
          else if (m < 6) fa[nks][m - 2] = rd_a(nch, nks, m - 2);
          else if (m < 10) fbb[nks][m - 4] = rd_b(nch, nks, m - 4);
        }
        if (fill) {
          if (m % 3 == 0) refill(m / 3);
          if (m == 11) { refill(4); if (tg >= 10) refill(5); }
        }
        if (hf == 17 && m == 1) hid128c(xst, rws, tid < 128 ? tid * 16 : OOB, WS_XST + P * 2048);
        SCHED();
      }
      if (ks == 0) WAITV(5);
      if (!light) {
        if (ks == 0 && ch >= 1) {
          constexpr int p = ch - 2;
          if constexpr (p >= 0 && p < 4) { A2a[0][p & 3] = lds128(L_MISC + w * 2048 + a2l); A2a[1][p & 3] = lds128(L_MISC + w * 2048 + (a2l ^ 64)); }
          if constexpr (p >= 4 && p < 8) { A2b[0][p & 3] = lds128(L_MISC + w * 2048 + a2l); A2b[1][p & 3] = lds128(L_MISC + w * 2048 + (a2l ^ 64)); }
        }
#pragma unroll
        for (int m = 12; m < 16; ++m) {
          mm(m);
          if (hf < 19 && m < 14) fbb[nks][m - 6] = rd_b(nch, nks, m - 6);
          if (fill && m == 14 && tg < 10) refill(5);
          SCHED();
        }
        if (ks == 0 && ch >= 1 && ch <= 8) { WAITL(); SCHED(); pair_in(ch - 1); }
      }
      if (hf == 1) {
        xstats();
      }
      if (hf == 9) {
        if (rp) {
          const rsrc_t rwout = mkrs(wout, 864 * 1280);
#pragma unroll
          for (int o = 0; o < 4; ++o)
            DMA(rwout, r8 * 1280 + (((lane & 7) ^ (2 * (g & 1) + 4 * (o & 1))) << 4), (rp_st * 32 + o * 8) * 1280 + rp_ln * 128, rp_lds + o * 1024);
        }
      }
      if (hf == 13) {
        if (rp) {
#pragma unroll
          for (int n = 0; n < 4; ++n) {
            const v4u f = ldstr2(rp_lds + g * 1024 + q4 * 128 + (((2 * n + (p4 >> 1)) ^ sig) << 4) + 8 * (p4 & 1));
            const u32 ct = 4 * rp_ln + n, c5 = ct / 5;
            st128c(f, rws, lane * 16, WS_WOB + ((c5 * 27 + rp_st) * 5 + (ct - c5 * 5)) * 1024);
          }
        }
        SCHED();
      }
      if (ks == 0) BAR();
    });
    NOP_OUT();
  }
  asm volatile("" ::"v"(touch0));
  opaque(xst);
  if (w < 2) {
#pragma unroll 1
    for (u32 spins = 0; spins < SPIN_MAX && !__all(xst.y == tl && xst.w == th); ++spins) {
      hid128c(xst, rws, tid * 16, WS_XST + P * 2048);
      asm volatile("s_waitcnt vmcnt(0)" : "+v"(xst)::"memory");
    }
    *(LAS u32*)(L_MEAN + tid * 4) = xst.x;
    *(LAS u32*)(L_RSTD + tid * 4) = xst.z;
  }
#pragma unroll
  for (int i = 0; i < 2; ++i)
    if (g == 0) *(LAS float*)(L_CW + (((light && i) ? 2 : (2 * i) ^ wr) * 4 + wc) * 64 + i16 * 4) = cwa[i][0];
  BAR();
  v4u zl[2];
  {
    const Consts kc = mkconsts();
    f4 mean[4], rstd[4];
    const u32 rowb = (64 * wr + 8 * g) * 4;
#pragma unroll
    for (int r = 0; r < 4; ++r) {
      mean[r] = ldsf4(L_MEAN + rowb + (r >> 1) * 128 + (r & 1) * 16);
      rstd[r] = ldsf4(L_RSTD + rowb + (r >> 1) * 128 + (r & 1) * 16);
    }
    float cw[4];
#pragma unroll
    for (int i = 0; i < 4; ++i) cw[i] = ldsf(L_CW + (((light && i >= 2) ? 2 : i ^ wr) * 4 + wc) * 64 + i16 * 4);
    const u32 st0 = P * 4 + 2 * wr;
    auto noact = [&](int i, int pr) {
      return pack8((acc[2 * pr][i] + mean[2 * pr] * cw[i]) * rstd[2 * pr], (acc[2 * pr + 1][i] + mean[2 * pr + 1] * cw[i]) * rstd[2 * pr + 1]);
    };
    const u32 rt0 = P * 8 + 4 * wr, vpq = (i16 + 16 * (g >> 1)) * 16 + (g & 1) * (40 * 512), vz1 = vpq + (g & 1) * (14 * 512);
    auto turn = [&](v4u f) {
      v4u sel[2];
      const u32 one = (i16 & 1) ? 0x3f800000u : 0x3f80u, e = (i16 >> 1) + 8 - 4 * g;
#pragma unroll
      for (u32 d = 0; d < 4; ++d) { sel[0][d] = e == 8 + d ? one : 0u; sel[1][d] = e == d ? one : 0u; }
      const f4 d0 = mfma(f, sel[0], f4{0.f, 0.f, 0.f, 0.f}), d1 = mfma(f, sel[1], f4{0.f, 0.f, 0.f, 0.f});
      const auto sx = __builtin_amdgcn_permlane16_swap(pk2(d0[0], d0[1]), pk2(d1[0], d1[1]), false, false);
      const auto sy = __builtin_amdgcn_permlane16_swap(pk2(d0[2], d0[3]), pk2(d1[2], d1[3]), false, false);
      return v4u{sx[0], sy[0], sx[1], sy[1]};
    };
    auto store_t = [&](v4u f, int pr, u32 t) {
      const bool isz1 = t >= 40;
      st128c(turn(f), rws, isz1 ? vz1 : vpq, isz1 ? WS_Z1 + ((rt0 + 2 * pr) * 54 + t - 40) * 512 : WS_PQ + ((rt0 + 2 * pr) * 40 + t) * 512);
    };
    auto store_n = [&](v4u f, int pr, u32 t) { st128c(f, rws, lane * 16, WS_KV + ((st0 + pr) * 80 + t - 148) * 1024); };
    if (light) {
      f4 pa[4], pb[4], zs[4], zq[4];
#pragma unroll
      for (int r = 0; r < 4; ++r) {
        pa[r] = rstd[r] * kc.c0[0];
        pb[r] = rstd[r] * rstd[r] * rstd[r] * (kc.c0[0] * 0.044715f);
      }
#pragma unroll
      for (int i = 0; i < 2; ++i) {
        if (vt[i] < 94) {
          store_t(noact(i, 0), 0, vt[i]);
          store_t(noact(i, 1), 1, vt[i]);
        }
#pragma unroll
        for (int r = 0; r < 4; ++r) {
          const f4 u = acc[r][i] + mean[r] * cw[i];
          const f4 p = pin(__builtin_elementwise_fma(u * u, pb[r], pa[r]) * u);
          const f4 d = pin(f4{__builtin_amdgcn_exp2f(p[0]), __builtin_amdgcn_exp2f(p[1]), __builtin_amdgcn_exp2f(p[2]), __builtin_amdgcn_exp2f(p[3])} + 1.0f);
          acc[r][i] = u * f4{__builtin_amdgcn_rcpf(d[0]), __builtin_amdgcn_rcpf(d[1]), __builtin_amdgcn_rcpf(d[2]), __builtin_amdgcn_rcpf(d[3])};
          opaque(acc[r][i]);
        }
        if (vt[i] < 94) {
#pragma unroll
          for (int r = 0; r < 4; ++r) { acc[r][i] = f4{0.f, 0.f, 0.f, 0.f}; opaque(acc[r][i]); }
        }
      }
#pragma unroll
      for (int r = 0; r < 4; ++r) {
        zs[r] = acc[r][0] + acc[r][1];
        zq[r] = acc[r][0] * acc[r][0] + acc[r][1] * acc[r][1];
      }
      const f4 ts = rowsum16x16(zs), tq = rowsum16x16(zq);
      if (p4 == 0) {
        *(LAS f4*)(L_ZS + wc * 512 + rowb + (q4 >> 1) * 128 + (q4 & 1) * 16) = ts;
        *(LAS f4*)(L_ZS + 2048 + wc * 512 + rowb + (q4 >> 1) * 128 + (q4 & 1) * 16) = tq;
      }
      WAITV(0);
      BAR();
      const u32 row = 64 * wr + lane;
      float ts1 = 0.f, tq1 = 0.f;
#pragma unroll
      for (int k = 0; k < 4; ++k) { ts1 += ldsf(L_ZS + k * 512 + row * 4); tq1 += ldsf(L_ZS + 2048 + k * 512 + row * 4); }
      const float rs1 = ldsf(L_RSTD + row * 4);
      ts1 *= rs1;
      tq1 *= rs1 * rs1;
      st128c(v4u{ub(ts1), tl, ub(tq1), th}, mkrs(ws, wc == 0 ? WS_END : 0), lane * 16, WS_STAT + (P * 7 + sid) * 2048 + wr * 1024);
      a2read<0>(A2a, w * 6144 + a2l);
      a2read<1>(A2b, w * 6144 + a2l);
      zl[0] = turn(noact(2, 0));
      zl[1] = turn(noact(2, 1));
      opaque(zl[0]); opaque(zl[1]);
      v4u gr[6];
#pragma unroll 1
      for (u32 spins = 0; spins < SPIN_MAX; ++spins) {
        asm volatile("" ::: "memory");
        bool ok = true;
#pragma unroll
        for (int k = 0; k < 6; ++k) {
          const u32 z = sid + 1 + k;
          gr[k] = ld128c(rws, lane * 16, WS_STAT + (P * 7 + (z >= 7 ? z - 7 : z)) * 2048 + wr * 1024);
        }
#pragma unroll
        for (int k = 0; k < 6; ++k) ok = ok && gr[k].y == tl && gr[k].w == th;
        if (__all(ok)) break;
      }
#pragma unroll
      for (int k = 0; k < 6; ++k) { ts1 += fb(gr[k].x); tq1 += fb(gr[k].z); }
      const float m2 = ts1 * (1.0f / 864.0f);
      const float r2 = __builtin_amdgcn_rsqf(fmaxf(tq1 * (1.0f / 864.0f) - m2 * m2, 0.f) + 1e-5f);
      const u32 lz = w * 6144 + 4096;
      *(LAS float*)(lz + lane * 4) = -m2 * r2;
      *(LAS float*)(lz + 256 + lane * 4) = r2;
#pragma unroll
      for (int r = 0; r < 4; ++r) {
        mean[r] = ldsf4(lz + g * 32 + (r >> 1) * 128 + (r & 1) * 16);
        rstd[r] *= ldsf4(lz + 256 + g * 32 + (r >> 1) * 128 + (r & 1) * 16);
      }
#pragma unroll
      for (int i = 0; i < 2; ++i) {
        const rsrc_t rz = mkrs(ws, vt[i] >= 94 ? WS_END : 0);
#pragma unroll
        for (int pr = 0; pr < 2; ++pr)
          st128c(pack8(acc[2 * pr][i] * rstd[2 * pr] + mean[2 * pr], acc[2 * pr + 1][i] * rstd[2 * pr + 1] + mean[2 * pr + 1]), rz, lane * 16,
                 WS_Z2N + ((st0 + pr) * 54 + vt[i] - 94) * 1024);
      }
      WAITL();
      WAITV(0);
    } else {
      f4 rl2[4];
#pragma unroll
      for (int r = 0; r < 4; ++r) rl2[r] = rstd[r] * 1.4426950408889634f;
#pragma unroll
      for (int i = 0; i < 4; ++i) {
        const u32 t = vt[i];
        v4u f[2];
        if (t < 40 || (t >= 148 && t < 188)) {
#pragma unroll
          for (int pr = 0; pr < 2; ++pr) {
            f4 y[2];
#pragma unroll
            for (int o = 0; o < 2; ++o) {
              const int r = 2 * pr + o;
              const f4 u = acc[r][i] + mean[r] * cw[i];
              y[o] = cat(phi2(lo2(u), lo2(rstd[r]), lo2(rl2[r]), kc), phi2(hi2(u), hi2(rstd[r]), hi2(rl2[r]), kc));
            }
            f[pr] = pack8(y[0], y[1]);
          }
        } else { f[0] = noact(i, 0); f[1] = noact(i, 1); }
        if (t < 94) { store_t(f[0], 0, t); store_t(f[1], 1, t); }
        else { store_n(f[0], 0, t); store_n(f[1], 1, t); }
      }
      WAITV(0);
    }
  }
  if (tid == 0) *(LAS u32*)L_CNT = 0u;
  BAR();
  if (tid == 0) set_flag(ws, WS_F1 + (P * 16 + sid) * 128, tag);
  if (light) {
#pragma unroll
    for (int pr = 0; pr < 2; ++pr)
      st128c(zl[pr], rws, (i16 + 16 * (g >> 1)) * 16 + (g & 1) * (54 * 512), WS_Z1 + ((P * 8 + 4 * wr + 2 * pr) * 54 + vt[2] - 40) * 512);
#pragma unroll
    for (int j = 0; j < 16; ++j) unit_blk(j >> 3, j & 7, 2 + (j >> 3));
  }

  constexpr int WIN = 11, JA = 33, JM = 39, Z1L = 42, Z1G = 52;
  static_assert(WIN >= 10 && WIN <= 11 && JA + 6 < 45 && JM + 5 < 45, "see the positions inside the loop");
  u32 v16 = lane * 16;
  const u32 zb = WS_Z2N + (8 * w * 54 + n0) * 1024;
  const rsrc_t rw7 = mkrs(ws, cc == 3 ? 0 : WS_END);
  auto ldz = [&](int i) { return ld128c((i % 7) == 6 ? rw7 : rws, v16 + ((i % 7) & 3) * 1024, zb + (((i / 7) ^ (2 * jh)) * 54 + ((i % 7) & 4)) * 1024); };
  v4u bw[WIN];
  const u32 zt = 94 + n0 + ((lane & 1) ? (cc == 3 ? 5 : 6) : 0) + ((lane & 4) ? ((lane & 1) ? -3 : 3) : 0);
  const u32 zfo = WS_F1 + ((2 * w + ((lane >> 1) & 1)) * 16 + owner(zt >> 2)) * 128;
  if (sid >= 7 && sid < 15) {
    const u32 hg = sid - 7, hp = w & 1, q = w >> 1;
    {
      const u32 t = 148 + 5 * hg + ((lane & 1) ? 4 : 0);
      wait_flags(ws, WS_F1 + (P * 16 + owner(t >> 2)) * 128, tag);
    }
    u64 zf;
    hid64c(zf, ws, zfo);
    v4u kf[3], vf[3];
#pragma unroll
    for (int a = 0; a < 3; ++a) {
      const u32 o = WS_KV + ((4 * P + q) * 80 + 5 * hg + 2 * hp) * 1024;
      kf[a] = ld128c(rws, v16 + a * 1024, o);
      vf[a] = ld128c(rws, v16 + a * 1024, o + 40 * 1024);
    }
    SCHED();
    const v4u ones = {0x3f803f80u, 0x3f803f80u, 0x3f803f80u, 0x3f803f80u};
    const f4 zero = {0.f, 0.f, 0.f, 0.f};
#pragma unroll
    for (int hh = 0; hh < 2; ++hh) {
      const u32 base = hh ? L_MISC + w * 2048 : w * 6144 + 4096;
#pragma unroll
      for (int a = hh; a < hh + 2; ++a) {
        const u32 dd = 16 * a + i16 - 8 * hp - 20 * hh;
#pragma unroll
        for (int e = hh; e < hh + 2; ++e) {
          const u32 e0 = 16 * e + 4 * g - 8 * hp - 20 * hh;
          const f4 d = mfma(vf[e], kf[a], zero);
          if (dd < 20 && e0 < 20) *(LAS f4*)(base + (dd * 20 + e0) * 4) = d;
        }
        const f4 dz = mfma(ones, kf[a], zero);
        if (dd < 20 && g == 0) *(LAS float*)(base + (400 + dd) * 4) = dz[0];
      }
    }
    SCHED();
    opaque(zf);
    if (!__all(zf == tag)) wait_flags(ws, zfo, tag);
    __builtin_amdgcn_fence(__ATOMIC_ACQUIRE, "wavefront");
#pragma unroll
    for (int i = 0; i < WIN; ++i) { bw[i] = ldz(i); SCHED(); }
    BAR();
    {
      const u32 hd = min(tid / 112, 3u), o = tid - hd * 112;
      const u32 src = (hd & 1) ? L_MISC + (hd >> 1) * 2048 + o * 16 : (hd >> 1) * 6144 + 4096 + o * 16, str = (hd & 1) ? 4096 : 12288;
      const f4 s = (ldsf4(src) + ldsf4(src + str)) + (ldsf4(src + 2 * str) + ldsf4(src + 3 * str));
      st128c(__builtin_bit_cast(v4u, s), rws, (tid < 448 && o < 105) ? (o < 100 ? WS_KVP + (P * 32 + 4 * hg) * 1792 + tid * 16 : WS_ZP + (hg * 16 + P) * 320 + (hd * 5 + o - 100) * 16) : OOB, 0);
    }
    SCHED();
  } else {
    wait_flags(ws, zfo, tag);
#pragma unroll
    for (int i = 0; i < WIN; ++i) { bw[i] = ldz(i); SCHED(); }
  }

  f4 acc2[7][4];
  const u32 mh = loc, mc = xcd;
  const u32 sB = w < 3 ? 4 * w : 3 * w + 3, nB = w < 3 ? 4 : 3;
  const u32 rtw = p64 * 4 + (w & 3), rt2 = p64 * 4 + ((w & 3) ^ cc);
  v4u wb[4][5];
  f2 z1f[4][2];
  u32 braw;
  u64 fl;
  auto redslot = [&](u32 ww, u32 id) { const u32 k = id >> 1; return (k < 6 ? STAGE + k * 1024 : (k < 12 ? 2 * STAGE + (k - 6) * 1024 : (k - 12) * 1024)) + ww * 6144 + (id & 1) * 512; };
  auto ldwb = [&](int s, int t) { return ld128c(rws, v16 + (t & 3) * 1024, WS_WOB + ((cs * 27 + sB + s) * 5 + (t & 4)) * 1024); };
  {
    u32 fo;
    {
      const u32 l = lane & 15, pn = p64 >> 1;
      const u32 tz = 40 + n0 + ((l & 1) ? (cc == 3 ? 5 : 6) : 0) + ((l & 2) ? ((l & 1) ? -3 : 3) : 0), tq = 5 * cs + ((l & 1) ? 4 : 0);
      const u32 s = (l >> 1) & 3;
      const u32 unit = (sB + (s >= nB ? 0 : s)) * 10 + ((5 * cs + ((l & 1) ? 4 : 0)) >> 2);
      const u32 li = unit / 3, xg = li / 3, sl3 = li - xg * 3, xc = xg >> 2;
      const u32 sidw = 4 * (xc & 1) + sl3;
      fo = l < 8 ? WS_F2 + (pn * 16 + owner((l < 4 ? tz : tq) >> 2)) * 128 : WS_F1 + ((4 * (xc >> 1) + (xg & 3)) * 16 + sidw) * 128;
      if (lane & 16) fo = WS_FKV + (l * 8 + (w < 4 ? cs : mh >> 2)) * 128;
    }
    opaque(fo); opaque(v16);
    auto a2rd = [&]() { u32 t = v16; opaque(t); return w * 6144 + (t & 240) * 8 + (((t >> 8) ^ ((t >> 5) & 7)) << 4); };
    v4u z1v[2], jr[9];
    f4 den[2];
    Consts kc;
    const u32 lz = w * 6144 + 4096;
    __builtin_amdgcn_s_setprio(3);
    NOP_IN();
    sfor<0, 56>([&](auto I) {
      constexpr int i = decltype(I)::v, st = i / 7, n = i % 7;
      const v4u cur = bw[i % WIN];
#pragma unroll
      for (int r = 0; r < 4; ++r) {
        if (st == 0) MF0(acc2[n][r], cur, A2a[0][r]);
        else if ((st >> 1) & 1) MF(acc2[n][r], cur, A2b[st & 1][r]);
        else MF(acc2[n][r], cur, A2a[st & 1][r]);
      }
      SCHED();
      if constexpr (i + WIN < 56) bw[i % WIN] = ldz(i + WIN);
      if constexpr (i == 3) {
        asm volatile("s_waitcnt vmcnt(4)\n\ts_barrier" ::: "memory");
        if (w == 0 && (v16 == 0 || (v16 == 16 && sid >= 7 && sid < 15))) set_flag(ws, v16 ? WS_FKV + (P * 8 + sid - 7) * 128 : WS_F2 + (P * 16 + sid) * 128, tag);
      }
      if constexpr (i == 13 || i == 27 || i == 41) __builtin_amdgcn_s_setprio(i == 13 ? 2 : (i == 27 ? 1 : 0));
      if constexpr (i == 6) {
#pragma unroll
        for (u32 o = 0; o < 2; ++o)
          if (w >= 4 && w + 4 * o < 10) DMA(mkrs(wo, 640 * 1280), (v16 >> 7) * 1280 + ((v16 & 112) ^ (((v16 >> 8) & 1) * 32 + (w & 2) * 32)),
              (mh * 20 + (((w >> 1) & 1) + 2 * o) * 8) * 1280 + ((mc * 160) & ~127u) + (w & 1) * 128, L_WO + ((w & 1) * 3 + ((w >> 1) & 1) + 2 * o) * 1024);
      }
      if constexpr (i == 13) a2read<2>(A2a, a2rd());
      if constexpr (i == 26 - WIN) hid64c(fl, ws, fo);
      if constexpr (i == Z1L) {
        const u32 vh = (v16 & 240) + ((v16 >> 1) & 256) + ((v16 & 256) << 2);
        const u32 vh1 = ((v16 & 256) && (w >= 4 || cc == 3)) ? OOB : vh + 2048;
        z1v[0] = ld128c(rws, vh, WS_Z1 + (rt2 * 54 + n0 + (w >> 2)) * 512);
        z1v[1] = ld128c(rws, vh1, WS_Z1 + (rt2 * 54 + n0 + (w >> 2)) * 512);
      }
      if constexpr (i == Z1G - 1) kc = mkconsts();
      if constexpr (i >= Z1G && i < Z1G + 4) {
        constexpr int j = (i - Z1G) >> 1, hh = (i - Z1G) & 1;
        const auto u = __builtin_amdgcn_permlane16_swap(z1v[j][hh], z1v[j][2 + hh], false, false);
#pragma unroll
        for (int k = 0; k < 2; ++k) {
          z1f[2 * j + k][hh] = gelu2(f2{blo(u[k]), bhi(u[k])}, kc);
          opaque(z1f[2 * j + k][hh]);
        }
      }
      if constexpr (i == 27) {
        opaque(fl);
        if (!__all(fl == tag)) wait_flags(ws, fo, tag);
        __builtin_amdgcn_fence(__ATOMIC_ACQUIRE, "wavefront");
#pragma unroll
        for (int r = 0; r < 9; ++r) {
          const u32 vo = w < 4 ? (r < 6 ? (v16 < (r < 5 ? 960 : 320) ? v16 : OOB) : ((r == 8 && v16 >= 512) ? OOB : v16)) : ((r < 8 && ((w & 1) == 0 || v16 < 576)) ? v16 : OOB);
          const u32 so = w < 4 ? (r < 6 ? WS_ZP + cs * 5120 + r * 960 : WS_PQ + (rtw * 40 + 5 * cs) * 512 + (r - 6) * 1024)
                               : WS_KVP + (((w & 2) * 4 + r) * 32 + mh) * 1792 + (w & 1) * 1024;
          jr[r] = ld128c(rws, vo, so);
        }
      }
      if constexpr (i == JA) {
        if (w < 4) {
          f4 s = __builtin_bit_cast(f4, jr[0]);
#pragma unroll
          for (int r = 1; r < 6; ++r) s += __builtin_bit_cast(f4, jr[r]);
          *(LAS f4*)(lz + v16) = s;
        }
      }
      if constexpr (i == JM) {
        if (w >= 4) {
          f4 s = __builtin_bit_cast(f4, jr[0]);
#pragma unroll
          for (int r = 1; r < 8; ++r) s += __builtin_bit_cast(f4, jr[r]);
          *(LAS f4*)(L_KVS + (w - 4) * 1024 + v16) = s;
          if (v16 == 0) __hip_atomic_fetch_add((LAS u32*)L_CNT, 1u, __ATOMIC_RELEASE, __HIP_MEMORY_SCOPE_WORKGROUP);
        }
      }
      if constexpr (i > JA && i <= JA + 3) {
        constexpr int s = i - JA - 1;
        if (w < 4) {
          u32 vv = v16;
          opaque(vv);
          const u32 ln = vv >> 4, g = ln >> 4, i16 = ln & 15;
          const u32 kq = 2 * (i16 >> 2) + (i16 & 1);
          const u32 m0 = (0x1Fu << (5 * (((kq + 4 * (i16 & 2)) * 52) >> 8))) >> (2 * g), m1 = (0x1Fu << (5 * (((kq + 16) * 52) >> 8))) >> (2 * g);
          const u32 za = lz + (32 * s + 8 * g) * 4;
          v4u zz = pack8(ldsf4(za) + ldsf4(za + 320) + ldsf4(za + 640), ldsf4(za + 16) + ldsf4(za + 336) + ldsf4(za + 656));
          if (s == 2 && g >= 2) zz = v4u{0u, 0u, 0u, 0u};
          const u32 a0 = __builtin_amdgcn_sbfe(m0, 8 * s, 1), a1 = __builtin_amdgcn_sbfe(m0, 8 * s + 1, 1);
          const u32 b0 = __builtin_amdgcn_sbfe(m1, 8 * s, 1), b1 = __builtin_amdgcn_sbfe(m1, 8 * s + 1, 1);
          const f4 eps = {1e-6f, 1e-6f, 1e-6f, 1e-6f};
          den[0] = mfma(v4u{zz.x & a0, zz.y & a0, zz.z & a1, zz.w & a1}, jr[6 + s], s == 0 ? eps : den[0]);
          den[1] = mfma(v4u{zz.x & b0, zz.y & b0, zz.z & b1, zz.w & b1}, jr[6 + s], s == 0 ? eps : den[1]);
        }
      }
      if constexpr (i > JA + 3 && i <= JA + 6) {
        constexpr int s = i - JA - 4;
        if (w < 4) {
          const float il = __builtin_amdgcn_rcpf(s < 2 ? den[0][2 * s] : den[1][0]), ih = __builtin_amdgcn_rcpf(s < 2 ? den[0][2 * s + 1] : den[1][1]);
          const v4u p = jr[6 + s];
          const f2 a = pin(f2{blo(p.x), bhi(p.x)} * il), bb = pin(f2{blo(p.y), bhi(p.y)} * il), c = pin(f2{blo(p.z), bhi(p.z)} * ih), d = pin(f2{blo(p.w), bhi(p.w)} * ih);
          st128c(v4u{pk2(a[0], a[1]), pk2(bb[0], bb[1]), pk2(c[0], c[1]), pk2(d[0], d[1])}, rws, (s == 2 && v16 >= 512) ? OOB : v16,
                 WS_AQ + (rtw * 40 + 5 * cs) * 512 + s * 1024);
        }
      }
      if constexpr (i == JM + 2 || i == JM + 3) {
        constexpr int s = i - JM - 2;
        if (w >= 4) {
          u32 vv = v16;
          opaque(vv);
          const u32 ln = vv >> 4, g = ln >> 4, i16 = ln & 15;
          if (s == 0) {
#pragma unroll 1
            for (u32 spins = 0; spins < SPIN_MAX && __hip_atomic_load((LAS u32*)L_CNT, __ATOMIC_ACQUIRE, __HIP_MEMORY_SCOPE_WORKGROUP) < 4; ++spins) {}
          }
          const u32 dd = s ? 16 + (i16 & 3) : i16;
          const u32 alo = L_KVS + (dd * 20 + 8 * (g < 3 ? g : 2)) * 4, ahi = L_KVS + (dd * 20 + 8 * (g < 2 ? g : 0) + 4) * 4;
          f4 lo = ldsf4(alo) + ldsf4(alo + 2048), hi = ldsf4(ahi) + ldsf4(ahi + 2048);
          if (g == 3) lo = f4{0.f, 0.f, 0.f, 0.f};
          if (g >= 2) hi = f4{0.f, 0.f, 0.f, 0.f};
          jr[6 + s] = pack8(lo, hi);
        }
      }
      if constexpr (i == JM + 4 || i == JM + 5) {
        constexpr int s = i - JM - 4;
        if (w >= 4 && (s == 0 || w == 4)) {
          u32 vv = v16;
          opaque(vv);
          const u32 ln = vv >> 4, g = ln >> 4, i16 = ln & 15;
          const u32 ct = w - 4 + 4 * s, cb = ((mc * 160) & 127u) + ct * 32, go = g < 3 ? g : 2, q4 = i16 >> 2, p4 = ln & 3;
          v4u bfr = ldstr2(L_WO + ((cb >> 7) * 3 + go) * 1024 + q4 * 128 + ((((cb & 127u) >> 4) + (p4 >> 1)) ^ (2 * ((q4 >> 1) & 1) + 4 * (go & 1))) * 16 + 8 * (p4 & 1));
          if (g == 3) bfr = v4u{0u, 0u, 0u, 0u};
#pragma unroll
          for (int a = 0; a < 2; ++a) {
            const f4 d = mfma(jr[6 + a], bfr, f4{0.f, 0.f, 0.f, 0.f});
            const u32 k = 20 * (mh & 3) + 16 * a + 4 * g;
            const u32 off = (k >> 4) * 512 + (i16 + 16 * ((k >> 3) & 1)) * 16 + (k & 4) * 2;
            st64c(v2u{pk2(d[0], d[1]), pk2(d[2], d[3])}, rws, (a == 0 || g == 0) ? off : OOB, WS_MB + ((mc * 8 + (mh >> 2)) * 5 + ct) * 2560);
          }
        }
      }
      if constexpr (i == 40) a2read<3>(A2b, a2rd());
      if constexpr (i == 45) braw = __builtin_amdgcn_raw_buffer_load_b16(mkrs(bsp, 4096), (v16 & 240) >> 3, rt2 * 32, 0);
      if constexpr (i >= 46) wb[(i - 46) / 5][(i - 46) % 5] = ldwb((i - 46) / 5, (i - 46) % 5);
      if constexpr (i >= 51) {
#pragma unroll
        for (int r = 0; r < 4; ++r) {
          const f4 a = acc2[i - 51][r];
          *(LAS v2u*)(redslot(w, (i - 51) * 4 + r) + (v16 >> 1)) = v2u{pk2(a[0], a[1]), pk2(a[2], a[3])};
        }
      }
      SCHED();
    });
  }
  NOP_OUT();
  {
    opaque(v16);
    const u32 lane = v16 >> 4, g = lane >> 4, i16 = lane & 15;
    const u32 v16h = g < 2 ? v16 : OOB;
    const u32 vh = (v16 & 240) + ((v16 >> 1) & 256) + ((v16 & 256) << 2), vh1 = ((v16 & 256) && (w >= 4 || cc == 3)) ? OOB : vh + 2048;
#pragma unroll
    for (int n = 5; n < 7; ++n)
#pragma unroll
      for (int r = 0; r < 4; ++r) {
        const f4 a = acc2[n][r];
        *(LAS v2u*)(redslot(w, n * 4 + r) + lane * 8) = v2u{pk2(a[0], a[1]), pk2(a[2], a[3])};
      }
    v4u sel;
    {
      const u32 one = g == (i16 >> 2) ? ((i16 & 1) ? 0x3f800000u : 0x3f80u) : 0u;
      sel.x = sel.z = (i16 & 2) ? 0u : one;
      sel.y = sel.w = (i16 & 2) ? one : 0u;
    }
    const float bias = blo(braw);
    SCHED();
    asm volatile("s_waitcnt vmcnt(10) lgkmcnt(0)\n\ts_barrier" ::: "memory");
    if (w == 0 && lane < 2) set_flag(ws, lane ? WS_FM + (mc * 32 + mh) * 128 : WS_FA + (p64 * 8 + cs) * 128, tag);
    const u32 fo2 = (lane & 4) ? WS_FA + (p64 * 8 + w) * 128 : WS_FM + (cs * 32 + 4 * w + (lane & 3)) * 128;
    {
      v2u pp[4][8];
#pragma unroll
      for (int k = 0; k < 4; ++k) {
        const u32 ro = redslot(0, min(w + 8 * k, 27u)) + lane * 8;
#pragma unroll
        for (int ww = 0; ww < 8; ++ww) pp[k][ww] = lds64(ro + ww * 6144);
      }
      SCHED();
#pragma unroll
      for (int t = 0; t < 5; ++t) wb[2][t] = ldwb(2, t);
      SCHED();
      v2u hv[4];
#pragma unroll
      for (int k = 0; k < 4; ++k) {
        f4 s = {bias, bias, bias, bias};
#pragma unroll
        for (int ww = 0; ww < 8; ww += 2) s = mfma(sel, v4u{pp[k][ww].x, pp[k][ww].y, pp[k][ww + 1].x, pp[k][ww + 1].y}, s);
        const f2 s0 = lo2(s) * z1f[k][0], s1 = hi2(s) * z1f[k][1];
        hv[k] = v2u{pk2(s0[0], s0[1]), pk2(s1[0], s1[1])};
      }
      v4u hw[2];
#pragma unroll
      for (int j = 0; j < 2; ++j) {
        const auto sx = __builtin_amdgcn_permlane16_swap(hv[2 * j].x, hv[2 * j + 1].x, false, false);
        const auto sy = __builtin_amdgcn_permlane16_swap(hv[2 * j].y, hv[2 * j + 1].y, false, false);
        hw[j] = v4u{sx[0], sy[0], sx[1], sy[1]};
        opaque(hw[j]);
      }
      SCHED();
      hid64c(fl, ws, fo2);
#pragma unroll
      for (int j = 0; j < 2; ++j) st128c(hw[j], rws, j ? vh1 : vh, WS_H + (rt2 * 54 + n0 + (w >> 2)) * 512);
    }
    SCHED();
    u32 fo3;
    {
      const u32 t = 2 * sB + ((lane & 1) ? 2 * nB - 1 : 0) + ((lane & 2) ? ((lane & 1) ? -3 : 3) : 0);
      const u32 hh = t >= 27 ? 1 : 0, tt = t - 27 * hh;
      fo3 = WS_FH + (p64 * 8 + 4 * hh + tt / 7) * 128;
    }
    SCHED();
    asm volatile("s_waitcnt vmcnt(2)" : "+v"(fl)::"memory");
    const bool early = __all(fl == tag);
    __builtin_amdgcn_fence(__ATOMIC_ACQUIRE, "wavefront");
    v4u aq[4][3], mb[5][3], hf[4][4];
    auto lda = [&](int r) {
#pragma unroll
      for (int s = 0; s < 3; ++s) aq[r][s] = ld128c(rws, s == 2 ? v16h : v16 + s * 1024, WS_AQ + ((p64 * 4 + r) * 40 + 5 * w) * 512 + (s == 2 ? 2048 : 0));
    };
    auto ldm = [&](int t) {
#pragma unroll
      for (int s = 0; s < 3; ++s) mb[t][s] = ld128c(rws, s == 2 ? v16h : v16, WS_MB + ((cs * 8 + w) * 5 + t) * 2560 + s * 1024);
    };
    auto ldh = [&](int r) {
#pragma unroll
      for (int s = 0; s < 3; ++s) hf[r][s] = ld128c(rws, v16 + s * 1024, WS_H + ((p64 * 4 + r) * 54 + 2 * sB) * 512);
      if (w < 3) hf[r][3] = ld128c(rws, v16 + 3072, WS_H + ((p64 * 4 + r) * 54 + 2 * sB) * 512);
    };
    if (early) {
      ldm(0); lda(0); lda(1);
      SCHED();
      WAITV(9);
    } else WAITV(0);
    asm volatile("s_barrier" ::: "memory");
    if (w == 0 && lane == 0) set_flag(ws, WS_FH + (p64 * 8 + cs) * 128, tag);
    if (!early) {
      wait_flags(ws, fo2, tag);
      ldm(0); lda(0); lda(1);
    }
    lda(2); lda(3); ldm(1); ldm(2);
    f4 acc3[5][4];
    v2u xr[3];
    u32 fo[3], fa[3];
    NOP_IN();
#pragma unroll
    for (int t = 0; t < 5; ++t) {
#pragma unroll
      for (int r = 0; r < 4; ++r) {
        MF0(acc3[t][r], mb[t][0], aq[r][0]);
        MF(acc3[t][r], mb[t][1], aq[r][1]);
        MF(acc3[t][r], mb[t][2], aq[r][2]);
      }
      SCHED();
      if (t < 2) ldm(t + 3);
      if (t == 1) hid64c(fl, ws, fo3);
      if (t == 2) {
        asm volatile("s_waitcnt vmcnt(0)" : "+v"(fl)::"memory");
        if (!__all(fl == tag)) wait_flags(ws, fo3, tag);
        __builtin_amdgcn_fence(__ATOMIC_ACQUIRE, "wavefront");
        if (w < 3) {
#pragma unroll
          for (int u = 0; u < 5; ++u) wb[3][u] = ldwb(3, u);
        }
      }
      if (t >= 2) ldh(t - 2);
      NOP_IN();
    }
#pragma unroll
    for (int r = 0; r < 4; ++r) {
#pragma unroll
      for (int s = 0; s < 3; ++s)
#pragma unroll
        for (int t = 0; t < 5; ++t) MF(acc3[t][r], wb[s][t], hf[r][s]);
      if (w < 3) {
#pragma unroll
        for (int t = 0; t < 5; ++t) MF(acc3[t][r], wb[3][t], hf[r][3]);
      }
      SCHED();
      if (r == 0) {
        ldh(3);
#pragma unroll
        for (int k = 0; k < 3; ++k) {
          const u32 it = lane + 64 * k, row = (it * 205) >> 12, qd = it - row * 20;
          fo[k] = it < 160 ? row * 1280 + qd * 8 : OOB;
          fa[k] = ((qd >> 2) * 4 + (w >> 1)) * 1024 + ((((8 * w + row) ^ qd) & 15) + 16 * (qd & 3)) * 16;
          xr[k] = __builtin_amdgcn_raw_buffer_load_b64(rx, fo[k], (p64 * 64 + 8 * w) * 1280 + cs * 160, 0);
        }
      }
      NOP_OUT();
#pragma unroll
      for (int t = 0; t < 5; ++t) *(LAS f4*)((w * 20 + t * 4 + r) * 1024 + (v16 ^ (((4 * t + g) & 15) << 4))) = acc3[t][r];
      SCHED();
    }
    BAR();
    {
      const rsrc_t ro = mkrs(out, 2048 * 1280);
#pragma unroll
      for (int k = 0; k < 3; ++k) {
        f4 s = {blo(xr[k].x), bhi(xr[k].x), blo(xr[k].y), bhi(xr[k].y)};
#pragma unroll
        for (int ww = 0; ww < 8; ++ww) s += ldsf4(ww * 20480 + fa[k]);
        __builtin_amdgcn_raw_buffer_store_b64(v2u{pk2(s[0], s[1]), pk2(s[2], s[3])}, ro, fo[k], (p64 * 64 + 8 * w) * 1280 + cs * 160, 16);
      }
    }
    if (b == 0 && w == 0 && lane == 0) set_flag(ws, WS_EPOCH, tag);
  }
}

extern "C" void kernel_launch(void* const* d_in, const int* in_sizes, int n_in, void* d_out, int out_size, void* d_ws, size_t ws_size,
                              hipStream_t stream) {
  mega<<<256, 512, 0, stream>>>((const u16*)d_in[0], (const u16*)d_in[1], (const u16*)d_in[2], (const u16*)d_in[4], (const u16*)d_in[5],
                                (const u16*)d_in[6], (const u16*)d_in[7], (u16*)d_out, (char*)d_ws);
}
```
